```python
import torch

def _softmax(x, axis=-1):
    return torch.softmax(x, dim=axis)
def _gelu(x):
    return torch.nn.functional.gelu(x, approximate="tanh")
def _silu(x):
    return torch.nn.functional.silu(x)
def _sigmoid(x):
    return torch.sigmoid(x)
def _layernorm(x, axis=-1, eps=1e-5):
    m = x.mean(dim=axis, keepdim=True)
    v = x.var(dim=axis, keepdim=True, unbiased=False)
    return (x - m) / torch.sqrt(v + eps)
def _scatter_add(tgt, idx, src, axis=0):
    return tgt.clone().index_add_(axis, idx, src)
def _segment_sum(src, seg, n):
    out = torch.zeros((n,) + src.shape[1:], dtype=src.dtype)
    return out.index_add_(0, seg, src)
def _pad(x, pw):
    flat = tuple(v for pair in reversed(pw) for v in pair)
    return torch.nn.functional.pad(x, flat)

kernel_name = 'sequential_preLN__ssm_conv_h3__glu_gated_glu__prompt'

def setup_inputs():

    _w = (lambda a: (lambda t: t.to(torch.bfloat16) if t.is_floating_point() else t)(torch.as_tensor(a)))
    def _A_setup():
        B, S, D = 1, 2048, 512
        ks = 4
        return {
            "q": _w((lambda *s: torch.randn(*s, dtype=torch.bfloat16) * 0.05)(B, S, D)),
            "k": _w((lambda *s: torch.randn(*s, dtype=torch.bfloat16) * 0.4)(B, S, D)),
            "v": _w((lambda *s: torch.randn(*s, dtype=torch.bfloat16) * 0.4)(B, S, D)),
            "shift_k": _w((lambda *s: torch.randn(*s, dtype=torch.bfloat16) * 0.4)(D, ks) * 0.5),
            "ssm_k": _w((lambda *s: torch.randn(*s, dtype=torch.bfloat16) * 0.4)(D, S) * (1.0 / S ** 0.5)),
            "ssm_D": _w((lambda *s: torch.randn(*s, dtype=torch.bfloat16) * 0.4)(D)),
        }
    _A_in = _A_setup()

    def _B_setup():
        B, S, D, F = 1, 2048, 512, 1376
        return {
            "x": _w((lambda *s: torch.randn(*s, dtype=torch.bfloat16) * 0.05)(B, S, D)),
            "w_gate": _w((lambda *s: torch.randn(*s, dtype=torch.bfloat16) * 0.05)(D, F)),
            "w_up": _w((lambda *s: torch.randn(*s, dtype=torch.bfloat16) * 0.05)(D, F)),
            "w_down": _w((lambda *s: torch.randn(*s, dtype=torch.bfloat16) * 0.05)(F, D)),
        }
    _B_in = _B_setup()

    return {
        "A_q": _A_in["q"],
        "A_k": _A_in["k"],
        "A_v": _A_in["v"],
        "A_shift_k": _A_in["shift_k"],
        "A_ssm_k": _A_in["ssm_k"],
        "A_ssm_D": _A_in["ssm_D"],
        "B_x": _B_in["x"],
        "B_w_gate": _B_in["w_gate"],
        "B_w_up": _B_in["w_up"],
        "B_w_down": _B_in["w_down"],
    }

def reference(A_q, A_k, A_v, A_shift_k, A_ssm_k, A_ssm_D, B_x, B_w_gate, B_w_up, B_w_down):
    def _A_ref(q, k, v, shift_k, ssm_k, ssm_D):
        B, S, D = q.shape
        ks = shift_k.shape[-1]
        k_pad = _pad(k, ((0, 0), (ks - 1, 0), (0, 0)))
        k_shift = torch.zeros_like(k)
        for t in range(ks):
            k_shift = k_shift + k_pad[:, t:t + S, :] * shift_k[:, ks - 1 - t][None, None, :]
        kv = k_shift * v
        L = 2 * S
        kv_pad = _pad(kv, ((0, 0), (0, S), (0, 0)))
        kf_pad = _pad(ssm_k, ((0, 0), (0, S)))
        kv_f = (lambda x, axes=(-2,-1): torch.fft.rfft2(x.float(), dim=axes))(kv_pad[:, None, :, :], axes=(1, 2))
        kf_f = (lambda x, axes=(-2,-1): torch.fft.rfft2(x.float(), dim=axes))(kf_pad[:, None, :], axes=(1, 2))
        kf_f = (lambda x, *d: x.permute(*d))(kf_f, 1, 2, 0)
        y_f = kv_f * kf_f[None, :, :, :]
        y = (lambda x, s, axes=(-2,-1): torch.fft.irfft2(x, s=s, dim=axes).to(torch.bfloat16))(y_f, (1, L), axes=(1, 2))[:, 0, :S, :]
        y = y + kv * ssm_D[None, None, :]
        return q * y

    def _B_ref(x, w_gate, w_up, w_down):
        g = torch.einsum("bsd,df->bsf", x, w_gate)
        u = torch.einsum("bsd,df->bsf", x, w_up)
        h = _sigmoid(g) * u
        return torch.einsum("bsf,fd->bsd", h, w_down)

    x = A_q
    h = x + _A_ref(_layernorm(x, axis=-1), A_k, A_v, A_shift_k, A_ssm_k, A_ssm_D)
    h2 = h + _B_ref(_layernorm(h, axis=-1), B_w_gate, B_w_up, B_w_down)
    return h2
```

```cpp
#include <hip/hip_runtime.h>
#include <hip/hip_bf16.h>
#include <stdint.h>

typedef unsigned short u16;
typedef unsigned int u32;
typedef unsigned long long u64;
typedef __attribute__((ext_vector_type(8))) __bf16 bf16x8;
typedef __attribute__((ext_vector_type(2))) __bf16 bf16x2;
typedef __attribute__((ext_vector_type(4))) float f32x4;
typedef __attribute__((ext_vector_type(2))) float f32x2;
typedef __attribute__((ext_vector_type(4))) u32 u32x4;
typedef __attribute__((ext_vector_type(2))) u32 u32x2;
#define LAS __attribute__((address_space(3)))
#define GAS __attribute__((address_space(1)))
typedef __amdgpu_buffer_rsrc_t rsrc_t;

constexpr int S = 2048, D = 512, F = 1376;
constexpr int NKS = F / 32;
constexpr int ST = S + 64;
constexpr float EPS = 1e-5f;
constexpr int AUX_NT = 2, AUX_SC1 = 16;

constexpr u32 WS_UR = 0;
constexpr u32 WS_XT = WS_UR + D * ST * 2;
constexpr u32 WS_HT = WS_XT + D * ST * 2;
constexpr u32 WS_ST = WS_HT + D * ST * 2;
constexpr u32 WS_ST2 = WS_ST + S * 16;
constexpr u32 WS_CS = WS_ST2 + S * 16;
constexpr u32 WS_HH = WS_CS + 32768;
constexpr u32 WS_FL = WS_HH + 128 * NKS * 1024;
constexpr u32 WS_END = WS_FL + 131072;
constexpr u32 FL_EPOCH = WS_FL, FL_U = WS_FL + 128, FL_X = FL_U + 256 * 128, FL_B = FL_X + 256 * 128,
              FL_BR = FL_B + 8 * 128, FL_C = FL_BR + 8 * 128, FL_TMO = FL_C + 16 * 128;

__device__ __forceinline__ float bflo(u32 p) { return __uint_as_float(p << 16); }
__device__ __forceinline__ float bfhi(u32 p) { return __uint_as_float(p & 0xffff0000u); }
__device__ __forceinline__ u32 pk2(float lo, float hi) {
  bf16x2 r = {(__bf16)lo, (__bf16)hi};
  return __builtin_bit_cast(u32, r);
}
__device__ __forceinline__ f32x4 mfma16(u32x4 a, u32x4 b, f32x4 c) {
  return __builtin_amdgcn_mfma_f32_16x16x32_bf16(__builtin_bit_cast(bf16x8, a), __builtin_bit_cast(bf16x8, b), c, 0, 0, 0);
}
__device__ __forceinline__ u32x2 lds_tr(u32 addr) {
  typedef __attribute__((ext_vector_type(4))) short v4s;
  v4s r = __builtin_amdgcn_ds_read_tr16_b64_v4i16((LAS v4s*)addr);
  return __builtin_bit_cast(u32x2, r);
}
__device__ __forceinline__ u32x4 lds_ld128(u32 addr) { return *(LAS u32x4*)addr; }
__device__ __forceinline__ u32x2 lds_ld64(u32 addr) { return *(LAS u32x2*)addr; }
__device__ __forceinline__ u32x2 lds_ld64v(u32 addr) { return *(volatile LAS u32x2*)addr; }
__device__ __forceinline__ void lds_st128(u32 addr, u32x4 v) { *(LAS u32x4*)addr = v; }
__device__ __forceinline__ void lds_st64(u32 addr, u32x2 v) { *(LAS u32x2*)addr = v; }
__device__ __forceinline__ u32 lds_base(void* p) { return (u32)(uintptr_t)(LAS char*)p; }

__device__ __forceinline__ rsrc_t mkrsrc(const void* p, u32 bytes) {
  return __builtin_amdgcn_make_buffer_rsrc((void*)p, 0, bytes, 0x00020000);
}
template <int AUX> __device__ __forceinline__ u32 bld32(rsrc_t r, u32 off) { return __builtin_amdgcn_raw_buffer_load_b32(r, off, 0, AUX); }
template <int AUX> __device__ __forceinline__ u32x4 bld128(rsrc_t r, u32 off) { return __builtin_amdgcn_raw_buffer_load_b128(r, off, 0, AUX); }
template <int AUX> __device__ __forceinline__ void bst128(rsrc_t r, u32 off, u32x4 v) { __builtin_amdgcn_raw_buffer_store_b128(v, r, off, 0, AUX); }

template <bool SC1> __device__ __forceinline__ void dma16(const void* sbase, u32 voff, u32 ldst) {
  if constexpr (SC1)
    asm volatile("s_mov_b32 m0, %2\n\ts_nop 0\n\tglobal_load_lds_dwordx4 %0, %1 sc1" :: "v"(voff), "s"(sbase), "s"(ldst) : "memory");
  else
    asm volatile("s_mov_b32 m0, %2\n\ts_nop 0\n\tglobal_load_lds_dwordx4 %0, %1" :: "v"(voff), "s"(sbase), "s"(ldst) : "memory");
}
__device__ __forceinline__ void dma4(const void* sbase, u32 voff, u32 ldst) {
  asm volatile("s_mov_b32 m0, %2\n\ts_nop 0\n\tglobal_load_lds_dword %0, %1" :: "v"(voff), "s"(sbase), "s"(ldst) : "memory");
}

template <int N> struct IC { static constexpr int value = N; };
template <int B, int E, class Fn> __device__ __forceinline__ void static_for(Fn&& fn) {
  if constexpr (B < E) { fn(IC<B>{}); static_for<B + 1, E>(fn); }
}
template <int N> __device__ __forceinline__ void wait_vm() { asm volatile("s_waitcnt vmcnt(%0)" :: "n"(N) : "memory"); }
__device__ __forceinline__ void bar() {
  asm volatile("" ::: "memory");
  __builtin_amdgcn_sched_barrier(0);
  __builtin_amdgcn_s_waitcnt(0xC07F);
  __builtin_amdgcn_s_barrier();
  __builtin_amdgcn_sched_barrier(0);
  asm volatile("" ::: "memory");
}

template <int CTRL, int RMASK> __device__ __forceinline__ float dpp_add(float x) {
  int y = __builtin_amdgcn_update_dpp(0, __float_as_int(x), CTRL, RMASK, 0xf, false);
  return x + __int_as_float(y);
}
__device__ __forceinline__ float wave_sum(float x) {
  x = dpp_add<0x111, 0xf>(x);
  x = dpp_add<0x112, 0xf>(x);
  x = dpp_add<0x114, 0xf>(x);
  x = dpp_add<0x118, 0xf>(x);
  x = dpp_add<0x142, 0xa>(x);
  x = dpp_add<0x143, 0xc>(x);
  return __int_as_float(__builtin_amdgcn_readlane(__float_as_int(x), 63));
}

constexpr int SPIN_CAP = 1 << 18;
__device__ __forceinline__ void st_word(char* ws, u32 off, u64 v) {
  __hip_atomic_store((GAS u64*)(ws + off), v, __ATOMIC_RELAXED, __HIP_MEMORY_SCOPE_AGENT);
}
__device__ __forceinline__ u64 add_word(char* ws, u32 off) {
  return __hip_atomic_fetch_add((GAS u64*)(ws + off), 1ull, __ATOMIC_RELAXED, __HIP_MEMORY_SCOPE_AGENT);
}
__device__ __forceinline__ u64 ld_word(char* ws, u32 off) {
  return __hip_atomic_load((GAS u64*)(ws + off), __ATOMIC_RELAXED, __HIP_MEMORY_SCOPE_AGENT);
}
__device__ __forceinline__ void poll_words(char* ws, u32 off, u32 stride, int n, u64 want, int lane) {
  for (int spins = 0;; ++spins) {
    u64 v = want;
    if (lane < n) v = ld_word(ws, off + lane * stride);
    if (__all(v == want)) break;
    if (spins > SPIN_CAP) { if (lane == 0) st_word(ws, FL_TMO, 1); break; }
    __builtin_amdgcn_s_sleep(1);
  }
}

__device__ __forceinline__ void lds_signal(u32 cnt, int lane) {
  asm volatile("" ::: "memory");
  if (lane == 0) __hip_atomic_fetch_add((LAS u32*)cnt, 1u, __ATOMIC_RELAXED, __HIP_MEMORY_SCOPE_WORKGROUP);
}
__device__ __forceinline__ void lds_wait(u32 cnt, u32 target) {
  for (int spins = 0; spins < (1 << 22); ++spins) {
    const u32 v = __hip_atomic_load((LAS u32*)cnt, __ATOMIC_RELAXED, __HIP_MEMORY_SCOPE_WORKGROUP);
    if (__builtin_amdgcn_readfirstlane(v) >= target) break;
    __builtin_amdgcn_s_sleep(1);
  }
  asm volatile("" ::: "memory");
}
__device__ __forceinline__ void role_bar(u32 cnt, u32 target, int lane) {
  asm volatile("" ::: "memory");
  __builtin_amdgcn_s_waitcnt(0xC07F);
  if (lane == 0) __hip_atomic_fetch_add((LAS u32*)cnt, 1u, __ATOMIC_RELAXED, __HIP_MEMORY_SCOPE_WORKGROUP);
  for (int spins = 0; spins < (1 << 22); ++spins) {
    const u32 v = __hip_atomic_load((LAS u32*)cnt, __ATOMIC_RELAXED, __HIP_MEMORY_SCOPE_WORKGROUP);
    if (__builtin_amdgcn_readfirstlane(v) >= target) break;
  }
  asm volatile("" ::: "memory");
}

constexpr int G1_STAGE = 10 * 2048, G1_NSLOT = 7, G1_NIT = D / 32;
constexpr int G1_WPRE = 7;
constexpr int G1_SPF = 4;
constexpr int CSTR = 4160;
constexpr int L_CPY = 3 * G1_STAGE;
constexpr int L_UR = L_CPY + 2 * 4 * CSTR;
constexpr int L_RED = L_UR + 2 * 4608;
constexpr int L_ST1 = L_RED + 16384;
constexpr int L_XR = L_ST1 + 32768;
constexpr int L_P2END = L_XR + 8192;
constexpr int L_SLAB = L_CPY;
constexpr int L_CSR = 163840 - 1024;
constexpr int L_TOUCH = 163840 - 512;
constexpr int L_CNT = 163840 - 256;
static_assert(L_P2END <= L_CSR, "P2 LDS");
constexpr int L_CS = 155648;
constexpr int L_ST2 = L_CS + 1024;
constexpr int L_CSRAW = 153600;
constexpr int L_MID = 147456;
static_assert(L_CS >= G1_NSLOT * G1_STAGE && L_ST2 + 2048 <= L_CSR, "P3 LDS");
constexpr int G2_RING = 36, G2_NST = 11, G2_PF = 4;
constexpr int L_RES = G2_RING * 4096;
constexpr int L_OX = 8 * 4096;
static_assert(L_RES + 8192 <= L_CS, "P4 LDS");
constexpr int g1_issued(int i) { return i == 0 ? 3 : 2 * i + 6 < G1_NIT ? 2 * i + 6 : G1_NIT; }
constexpr int g2_issued(int s) { return s + G2_PF < G2_NST ? s + G2_PF : G2_NST; }
static_assert(G1_NSLOT == 7 && G1_WPRE >= 3 && G1_WPRE <= G1_NSLOT, "slot reuse schedule of the P3 ring");
constexpr int CODE_LINES = 306, CODE_FIRST = 16, EARLY_L = 172;

__global__ __launch_bounds__(512, 2) void fused(const u16* __restrict__ q, const u16* __restrict__ k, const u16* __restrict__ v,
                                             const u16* __restrict__ shift_k, const u16* __restrict__ ssm_k,
                                             const u16* __restrict__ ssm_D, const u16* __restrict__ wg,
                                             const u16* __restrict__ wu, const u16* __restrict__ wd,
                                             u16* __restrict__ out, char* __restrict__ ws) {
  __shared__ __attribute__((aligned(16))) char smem[163840];
  constexpr int SCP = AUX_SC1;
  const u32 sm = lds_base(smem);
  const int b = blockIdx.x, xcd = b & 7, jj = b >> 3;
  const int tid = threadIdx.x, l = tid & 63;
  const int w8 = __builtin_amdgcn_readfirstlane(tid >> 6);
  const bool loader = w8 >= 4;
  const int w = w8 & 3;
  const int li = l & 15, g = l >> 4;
  const rsrc_t rws = mkrsrc(ws, WS_END);
  asm volatile("" :: "s"(q), "s"(k), "s"(v), "s"(shift_k), "s"(ssm_k), "s"(ssm_D), "s"(wg), "s"(wu), "s"(wd), "s"(out), "s"(ws));
  if (tid < 8) *(LAS u32*)(sm + L_CNT + tid * 4) = 0;
  const u32 rcnt = sm + L_CNT + (loader ? 4 : 0);
  u64 tag, cbase;
  u64 pc0;
  asm volatile("s_getpc_b64 %0" : "=s"(pc0));
  pc0 &= ~(u64)127;
  const int dpair = xcd * 32 + jj;
  const int mt1 = 8 * (xcd & 1) + (jj & 7), nt1 = 4 * (xcd >> 1) + (jj >> 3);
  const bool g1_live = nt1 < 15;
  const int mt2 = 8 * (xcd >> 1) + (jj & 7), nt2 = 4 * (xcd & 1) + (jj >> 3);

  u32x4 fw[22][2];
  auto g2_read_w = [&](int idx, int slot) {
    const int qq = li >> 2, p = l & 3;
#pragma unroll
    for (int i = 0; i < 2; ++i) {
      const u32 a0 = sm + slot * 4096 + 2048 * (w & 1) + 512 * (g >> 1) + 64 * (4 * (g & 1) + qq) + 16 * (p ^ g) + 8 * i;
      const u32x2 a = lds_tr(a0), c = lds_tr(a0 + 1024);
      fw[idx][i] = u32x4{a[0], a[1], c[0], c[1]};
    }
  };

  const int wf = w & 1, wt = w >> 1;
  auto glu = [&](int n, const f32x4 st, const f32x4 (&ag)[3], const f32x4 (&au)[3], const f32x4 (&csg)[3], const f32x4 (&csu)[3], u32 (&pk)[6]) {
    constexpr float NL2E = -1.4426950408889634f;
    const int tb = 4 * wt + ((n + 2 * wf) & 3);
    const float rs = st[0], irs = __builtin_amdgcn_rcpf(rs);
    const f32x2 nm2 = {-st[2], -st[2]}, rg2 = {rs * NL2E, rs * NL2E}, irs2 = {irs, irs};
    f32x2 x[6], y[6];
    auto pin = [&](f32x2 (&v)[6]) {
      asm volatile("" : "+v"(v[0]), "+v"(v[1]), "+v"(v[2]), "+v"(v[3]), "+v"(v[4]), "+v"(v[5]));
      __builtin_amdgcn_sched_barrier(0);
    };
#pragma unroll
    for (int i = 0; i < 6; ++i) {
      const f32x2 a = {ag[i >> 1][2 * (i & 1)], ag[i >> 1][2 * (i & 1) + 1]};
      const f32x2 c = {csg[i >> 1][2 * (i & 1)], csg[i >> 1][2 * (i & 1) + 1]};
      x[i] = __builtin_elementwise_fma(nm2, c, a);
    }
    pin(x);
#pragma unroll
    for (int i = 0; i < 6; ++i) x[i] = x[i] * rg2;
    pin(x);
#pragma unroll
    for (int i = 0; i < 6; ++i) x[i] = f32x2{__builtin_amdgcn_exp2f(x[i][0]), __builtin_amdgcn_exp2f(x[i][1])};
#pragma unroll
    for (int i = 0; i < 6; ++i) {
      const f32x2 a = {au[i >> 1][2 * (i & 1)], au[i >> 1][2 * (i & 1) + 1]};
      const f32x2 c = {csu[i >> 1][2 * (i & 1)], csu[i >> 1][2 * (i & 1) + 1]};
      y[i] = __builtin_elementwise_fma(nm2, c, a);
    }
    pin(x); pin(y);
#pragma unroll
    for (int i = 0; i < 6; ++i) x[i] = __builtin_elementwise_fma(x[i], irs2, irs2);
    pin(x);
#pragma unroll
    for (int i = 0; i < 6; ++i) x[i] = f32x2{__builtin_amdgcn_rcpf(x[i][0]), __builtin_amdgcn_rcpf(x[i][1])};
    pin(x);
#pragma unroll
    for (int i = 0; i < 6; ++i) y[i] = y[i] * x[i];
    pin(y);
#pragma unroll
    for (int i = 0; i < 6; ++i) pk[i] = pk2(y[i][0], y[i][1]);
    const int ks_pair = 3 * nt1 + 2 * wf;
    bst128<SCP>(rws, WS_HH + (u32)((8 * mt1 + tb) * NKS) * 1024 + l * 16 + (ks_pair < NKS ? (u32)ks_pair * 1024 : 0x80000000u),
                u32x4{pk[2], pk[3], pk[4], pk[5]});
    __builtin_amdgcn_sched_barrier(0);
  };

  if (loader) {
    const int t4 = tid & 255;
    const int cp = l & 31, rg = 2 * w + (l >> 5);
    const int d0 = xcd * 64 + 2 * cp, s0 = jj * 64 + 8 * rg;
    const rsrc_t rq = mkrsrc(q, S * D * 2);
    u32 xx[8];
#pragma unroll
    for (int j = 0; j < 8; ++j) xx[j] = bld32<AUX_NT>(rq, (u32)(((s0 + j) * D + d0) * 2));
    u32x4 klo[2], khi[2];
    {
      const rsrc_t rs = mkrsrc(ssm_k, D * S * 2);
#pragma unroll
      for (int cc = 0; cc < 2; ++cc) {
        klo[cc] = bld128<AUX_NT>(rs, (u32)(((dpair * 2 + cc) * S + t4 * 8) * 2));
        khi[cc] = bld128<AUX_NT>(rs, (u32)(((dpair * 2 + cc) * S + (t4 < 255 ? t4 * 8 + 8 : t4 * 8)) * 2));
      }
    }
    tag = ld_word(ws, FL_EPOCH) + 1;
    cbase = tag << 8;
    if (w == 3) {
      dma4((const char*)pc0, (u32)(8 + l) * 128, sm + L_TOUCH);
      dma4((const char*)pc0, (u32)(EARLY_L + l) * 128, sm + L_TOUCH);
    }
    bar();
    u32 g1_voff[5], g1_ldst[5];
    const char* g1_sptr[5];
#pragma unroll
    for (int i = 0; i < 5; ++i) {
      if (i < 2) {
        const int row = 8 * w + ((l >> 2) & 7), ch = (l & 3) ^ ((row >> 2) & 3);
        int col = nt1 * 96 + 32 * (l >> 5) + 8 * ch;
        col = col > F - 8 ? F - 8 : col;
        g1_voff[i] = (u32)(row * F + col) * 2;
        g1_sptr[i] = (const char*)(i ? wu : wg);
        g1_ldst[i] = sm + i * 6144 + w * 1024;
      } else if (i == 2) {
        const int row = 16 * (w & 1) + (l >> 2), ch = (l & 3) ^ ((row >> 2) & 3);
        int col = nt1 * 96 + 64 + 8 * ch;
        col = col > F - 8 ? F - 8 : col;
        g1_voff[i] = (u32)(row * F + col) * 2;
        g1_sptr[i] = (const char*)((w >> 1) ? wu : wg);
        g1_ldst[i] = sm + (w >> 1) * 6144 + 4096 + (w & 1) * 1024;
      } else {
        const int sp = i - 3, row = 8 * w + ((l >> 2) & 7), ch = (l & 3) ^ ((row >> 2) & 3);
        g1_voff[i] = (u32)(row * ST + mt1 * 128 + 64 * sp + 32 * (l >> 5) + 8 * ch) * 2;
        g1_sptr[i] = (const char*)ws + WS_HT;
        g1_ldst[i] = sm + 12288 + w * 2048 + sp * 1024;
      }
    }
    auto g1_stage_w = [&](int it) {
#pragma unroll
      for (int i = 0; i < 3; ++i)
        dma16<false>(g1_sptr[i] + (size_t)it * (32 * F * 2), g1_voff[i], g1_ldst[i] + (it % G1_NSLOT) * G1_STAGE);
    };
    auto g1_stage_h = [&](int it) {
#pragma unroll
      for (int i = 3; i < 5; ++i)
        dma16<true>(g1_sptr[i] + (size_t)it * (32 * ST * 2), g1_voff[i], g1_ldst[i] + (it % G1_NSLOT) * G1_STAGE);
    };
    const u32 g2_woff = (u32)((16 * (w & 1) + (l >> 2)) * D + nt2 * 64 + 32 * (w >> 1) + 8 * ((l & 3) ^ (((16 * (w & 1) + (l >> 2)) >> 2) & 3))) * 2;
    auto g2_stage_w = [&](int ks, int slot) { dma16<false>((const char*)wd + (size_t)ks * 32 * D * 2, g2_woff, sm + slot * 4096 + w * 1024); };
    {
      u32x4 o0, o1;
#pragma unroll
      for (int j = 0; j < 4; ++j) {
        o0[j] = __builtin_amdgcn_perm(xx[2 * j + 1], xx[2 * j], 0x05040100u);
        o1[j] = __builtin_amdgcn_perm(xx[2 * j + 1], xx[2 * j], 0x07060302u);
      }
      const u32 tb = sm + L_RED + 8192;
      lds_st128(tb + (2 * cp) * 128 + ((rg ^ (cp & 7)) * 16), o0);
      lds_st128(tb + (2 * cp + 1) * 128 + ((rg ^ (cp & 7)) * 16), o1);
      role_bar(rcnt, 4, l);
#pragma unroll
      for (int i = 0; i < 2; ++i) {
        const int dl = 32 * i + (t4 >> 3), c = t4 & 7;
        const u32x4 val = lds_ld128(tb + dl * 128 + ((c ^ ((dl >> 1) & 7)) * 16));
        bst128<SCP>(rws, WS_XT + jj * 128 + (u32)((xcd * 64 + dl) * ST * 2 + c * 16), val);
      }
    }
    asm volatile("" ::: "memory");
    const int r0 = b * 8 + w * 2;
    const u32x4 xa = bld128<AUX_NT>(rq, (u32)((r0 * D + l * 8) * 2));
    const u32x4 xb = bld128<AUX_NT>(rq, (u32)(((r0 + 1) * D + l * 8) * 2));
#pragma unroll
    for (int cc = 0; cc < 2; ++cc) {
      const u32 cb = sm + L_CPY + cc * 4 * CSTR + (t4 + 2) * 16;
      const u32 dw[6] = {klo[cc][0], klo[cc][1], klo[cc][2], klo[cc][3], khi[cc][0], khi[cc][1]};
      u32 od[5];
#pragma unroll
      for (int j = 0; j < 5; ++j) od[j] = __builtin_amdgcn_alignbyte(dw[j + 1], dw[j], 2);
      lds_st128(cb + 0 * CSTR, u32x4{dw[0], dw[1], dw[2], dw[3]});
      lds_st128(cb + 1 * CSTR, u32x4{od[0], od[1], od[2], od[3]});
      lds_st128(cb + 2 * CSTR, u32x4{dw[1], dw[2], dw[3], dw[4]});
      lds_st128(cb + 3 * CSTR, u32x4{od[1], od[2], od[3], od[4]});
      if (t4 == 0) {
        const u32 c0 = sm + L_CPY + cc * 4 * CSTR;
        const u32x4 z = {0, 0, 0, 0};
#pragma unroll
        for (int e = 0; e < 4; ++e) lds_st128(c0 + e * CSTR, z);
        lds_st128(c0 + 0 * CSTR + 16, z);
        lds_st128(c0 + 1 * CSTR + 16, u32x4{0, 0, 0, dw[0] << 16});
        lds_st128(c0 + 2 * CSTR + 16, u32x4{0, 0, 0, dw[0]});
        lds_st128(c0 + 3 * CSTR + 16, u32x4{0, 0, dw[0] << 16, od[0]});
      }
      if (t4 < 32) lds_st128(sm + L_UR + cc * 4608 + 4096 + t4 * 16, u32x4{0, 0, 0, 0});
    }
    wait_vm<2>();
    role_bar(rcnt, 8, l);
    if (w == 0 && l == 0) st_word(ws, FL_X + b * 128, tag);
    {
      float sa = 0.f, sb = 0.f;
#pragma unroll
      for (int j = 0; j < 4; ++j) {
        sa += bflo(xa[j]) + bfhi(xa[j]);
        sb += bflo(xb[j]) + bfhi(xb[j]);
      }
      const float ma = wave_sum(sa) * (1.f / D), mb = wave_sum(sb) * (1.f / D);
      float va = 0.f, vb = 0.f;
#pragma unroll
      for (int j = 0; j < 4; ++j) {
        float a = bflo(xa[j]) - ma, c = bfhi(xa[j]) - ma;
        va = fmaf(a, a, fmaf(c, c, va));
        a = bflo(xb[j]) - mb; c = bfhi(xb[j]) - mb;
        vb = fmaf(a, a, fmaf(c, c, vb));
      }
      va = wave_sum(va) * (1.f / D);
      vb = wave_sum(vb) * (1.f / D);
      const float ra = rsqrtf(va + EPS), rb = rsqrtf(vb + EPS);
      if (l < 4) {
        const float val = l == 0 ? ra : l == 1 ? -ma * ra : l == 2 ? rb : -mb * rb;
        st_word(ws, WS_ST + r0 * 16 + l * 8, ((u64)(u32)tag << 32) | __float_as_uint(val));
      }
    }
    const int isu = xcd & 1, blk = 24 * (xcd >> 1) + jj;
    const bool do_cs = jj < 24 && blk < F / 16;
    bar();
    if (w == 0) poll_words(ws, FL_X + xcd * 128, 8 * 128, 32, tag, l);
    role_bar(rcnt, 12, l);
#pragma unroll
    for (int i = 0; i < 2; ++i)
      dma16<true>(ws + WS_XT + (size_t)(dpair * 2 + (w >> 1)) * ST * 2 + (2 * (w & 1) + i) * 1024, l * 16,
                  sm + L_XR + (w >> 1) * 4096 + (2 * (w & 1) + i) * 1024);
    for (int spins = 0;; ++spins) {
#pragma unroll
      for (int i = 0; i < 8; ++i) dma16<true>(ws + WS_ST + (8 * w + i) * 1024, l * 16, sm + L_ST1 + (8 * w + i) * 1024);
      wait_vm<0>();
      bool ok = true;
      u32x4 rec[8];
#pragma unroll
      for (int i = 0; i < 8; ++i) {
        rec[i] = lds_ld128(sm + L_ST1 + (8 * w + i) * 1024 + l * 16);
        ok = ok && rec[i][1] == (u32)tag && rec[i][3] == (u32)tag;
      }
      if (__all(ok) || spins > SPIN_CAP) {
        if (spins > SPIN_CAP && l == 0) st_word(ws, FL_TMO, 3);
#pragma unroll
        for (int i = 0; i < 8; ++i) {
          *(LAS u32*)(sm + L_ST1 + w * 8192 + (i * 64 + l) * 4) = rec[i][0];
          *(LAS u32*)(sm + L_ST1 + w * 8192 + 2048 + (i * 64 + l) * 4) = rec[i][2];
        }
        break;
      }
      __builtin_amdgcn_s_sleep(4);
    }
    bar();
    if (do_cs) {
#pragma unroll
      for (int i = 0; i < 4; ++i)
        dma16<false>(isu ? wu : wg, (u32)((128 * w + 32 * i + (l >> 1)) * F + blk * 16 + (l & 1) * 8) * 2, sm + L_SLAB + (4 * w + i) * 1024);
    }
    asm volatile("" ::: "memory");
    if (g1_live) static_for<0, 3>([&](auto I) { g1_stage_w(I.value); });
    bar();
    if (do_cs) {
      if (g1_live) wait_vm<9>(); else wait_vm<0>();
      f32x4 cacc = {0, 0, 0, 0};
#pragma unroll
      for (int i = 0; i < 4; ++i) {
        const u32 a = sm + L_SLAB + (4 * w + i) * 1024 + (8 * g + (li >> 2)) * 32 + (l & 3) * 8;
        const u32x2 lo = lds_tr(a), hi = lds_tr(a + 128);
        cacc = mfma16(u32x4{0x3f803f80u, 0x3f803f80u, 0x3f803f80u, 0x3f803f80u}, u32x4{lo[0], lo[1], hi[0], hi[1]}, cacc);
      }
      if (l < 16) *(LAS float*)(sm + L_CSR + w * 64 + l * 4) = cacc[0];
    }
    role_bar(rcnt, 16, l);
    if (do_cs && w == 0 && l < 16) {
      float s = 0.f;
#pragma unroll
      for (int ww = 0; ww < 4; ++ww) s += *(LAS float*)(sm + L_CSR + ww * 64 + l * 4);
      st_word(ws, WS_CS + (u32)((isu ? F : 0) + blk * 16 + l) * 8, ((u64)(u32)tag << 32) | __float_as_uint(s));
    }
    if (g1_live) static_for<3, G1_WPRE>([&](auto I) { g1_stage_w(I.value); });
    bar();
    if (g1_live) {
      const int f0 = nt1 * 96;
      static_for<0, 3>([&](auto I) { g1_stage_h(I.value); });
      wait_vm<4>();
      bar();
      const char* st2 = ws + WS_ST2 + (mt1 * 128 + (w & 1) * 64) * 16;
      const u32 cs_off = (u32)(((w & 1) * F + f0 + (2 * l < 96 && f0 + 2 * l < F ? 2 * l : 0)) * 8);
      static_for<0, 8>([&](auto I) {
        constexpr int i = I.value;
        constexpr int need = 2 * i + 2 < G1_NIT ? 2 * i + 2 : G1_NIT - 1;
        constexpr int issued = g1_issued(i) - 1;
        constexpr int full_lo = need + 1 > G1_WPRE ? need + 1 : G1_WPRE;
        constexpr int younger = 2 * (issued - need) + 3 * (issued + 1 > full_lo ? issued + 1 - full_lo : 0) + (i > 5 ? 12 : 0) + (i > 6 ? 10 : 0);
        wait_vm<younger>();
        bar();
        static_for<g1_issued(i), g1_issued(i + 1)>([&](auto J) {
          if constexpr (J.value >= G1_WPRE) g1_stage_w(J.value);
          g1_stage_h(J.value);
        });
        if constexpr (i == 5) {
          dma16<true>(st2, l * 16, sm + L_ST2 + (w & 1) * 1024);
          dma16<true>(ws + WS_CS, cs_off, sm + L_CSRAW + (w & 1) * 1024);
          static_for<0, 10>([&](auto J) { g2_stage_w(2 * J.value + 1, 10 + J.value); });
        }
        if constexpr (i == 6) static_for<10, 20>([&](auto J) { g2_stage_w(2 * J.value + 1, 10 + J.value); });
        if constexpr (i == 7) g2_stage_w(41, 30);
      });
      wait_vm<21>();
      for (int spins = 0;; ++spins) {
        const u32x4 rec = lds_ld128(sm + L_ST2 + (w & 1) * 1024 + l * 16);
        const u32x4 csr = lds_ld128(sm + L_CSRAW + (w & 1) * 1024 + l * 16);
        if (__all(rec[1] == (u32)tag && rec[3] == (u32)tag && csr[1] == (u32)tag && csr[3] == (u32)tag) || spins > SPIN_CAP) {
          if (spins > SPIN_CAP && l == 0) st_word(ws, FL_TMO, 4);
          if (w < 2 && l < 48) lds_st64(sm + L_CS + w * 384 + l * 8, u32x2{csr[0], csr[2]});
          break;
        }
        __builtin_amdgcn_s_sleep(4);
        dma16<true>(st2, l * 16, sm + L_ST2 + (w & 1) * 1024);
        dma16<true>(ws + WS_CS, cs_off, sm + L_CSRAW + (w & 1) * 1024);
        wait_vm<0>();
      }
      bar();
    } else {
      static_for<0, G1_SPF>([&](auto I) { g1_stage_h(I.value); });
      static_for<0, G1_NIT>([&](auto I) {
        constexpr int it = I.value;
        constexpr int issued = it + G1_SPF < G1_NIT ? it + G1_SPF : G1_NIT;
        wait_vm<2 * (issued - 1 - it)>();
        bar();
        if constexpr (it + G1_SPF < G1_NIT) g1_stage_h(it + G1_SPF);
      });
      bar();
      static_for<0, 21>([&](auto J) { g2_stage_w(2 * J.value + 1, 10 + J.value); });
    }
    static_for<0, 10>([&](auto I) { g2_stage_w(2 * I.value, I.value); });
    wait_vm<10>();
    role_bar(rcnt, 20, l);
    static_for<0, 21>([&](auto I) { g2_read_w(I.value, 10 + I.value); });
    fw[21][0] = fw[21][1] = u32x4{0, 0, 0, 0};
    role_bar(rcnt, 24, l);
    lds_wait(sm + L_CNT + 8, 4);
    static_for<10, 22>([&](auto I) { g2_stage_w(2 * I.value, I.value); });
    wait_vm<0>();
    bar();
#pragma unroll
    for (int i = 0; i < 2; ++i) {
      const int row = 8 * (2 * w + i) + ((l >> 2) & 7), ch = (l & 3) ^ ((row >> 2) & 3);
      dma16<true>(ws + WS_HT, (u32)((nt2 * 64 + row) * ST + mt2 * 64 + 32 * (l >> 5) + 8 * ch) * 2, sm + L_RES + (2 * w + i) * 1024);
    }
    bar();
  } else {
    float dD;
    {
      const int cp = l & 31, rg = 2 * w + (l >> 5);
      const int d0 = xcd * 64 + 2 * cp, s0 = jj * 64 + 8 * rg;
      const rsrc_t rk = mkrsrc(k, S * D * 2), rv = mkrsrc(v, S * D * 2);
      u32 kk[11], vv[8];
#pragma unroll
      for (int j = 0; j < 11; ++j) kk[j] = bld32<AUX_NT>(rk, (u32)(((s0 - 3 + j) * D + d0) * 2));
#pragma unroll
      for (int j = 0; j < 8; ++j) vv[j] = bld32<AUX_NT>(rv, (u32)(((s0 + j) * D + d0) * 2));
      const u32x4 wk = *(const u32x4*)(shift_k + d0 * 4);
      const u32 dDraw = ssm_D[dpair * 2 + (w >> 1)];
      tag = ld_word(ws, FL_EPOCH) + 1;
      cbase = tag << 8;
      bar();
      dD = bflo(dDraw);
      const float w0[4] = {bflo(wk[0]), bfhi(wk[0]), bflo(wk[1]), bfhi(wk[1])};
      const float w1[4] = {bflo(wk[2]), bfhi(wk[2]), bflo(wk[3]), bfhi(wk[3])};
      float u0[8], u1[8];
#pragma unroll
      for (int j = 0; j < 8; ++j) {
        float a0 = 0.f, a1 = 0.f;
#pragma unroll
        for (int t = 0; t < 4; ++t) {
          a0 = fmaf(bflo(kk[j + 3 - t]), w0[t], a0);
          a1 = fmaf(bfhi(kk[j + 3 - t]), w1[t], a1);
        }
        u0[j] = a0 * bflo(vv[j]);
        u1[j] = a1 * bfhi(vv[j]);
      }
      const u32x4 o0 = {pk2(u0[7], u0[6]), pk2(u0[5], u0[4]), pk2(u0[3], u0[2]), pk2(u0[1], u0[0])};
      const u32x4 o1 = {pk2(u1[7], u1[6]), pk2(u1[5], u1[4]), pk2(u1[3], u1[2]), pk2(u1[1], u1[0])};
      const u32 tb = sm + L_RED;
      lds_st128(tb + (2 * cp) * 128 + (((7 - rg) ^ (cp & 7)) * 16), o0);
      lds_st128(tb + (2 * cp + 1) * 128 + (((7 - rg) ^ (cp & 7)) * 16), o1);
      role_bar(rcnt, 4, l);
      const int tt = tid & 255;
#pragma unroll
      for (int i = 0; i < 2; ++i) {
        const int dl = 32 * i + (tt >> 3), c = tt & 7;
        const u32x4 val = lds_ld128(tb + dl * 128 + ((c ^ ((dl >> 1) & 7)) * 16));
        bst128<SCP>(rws, WS_UR + (1984 - 64 * jj) * 2 + (u32)((xcd * 64 + dl) * ST * 2 + c * 16), val);
      }
      if (tid == 0) {
        if (jj == 0) st_word(ws, FL_B + xcd * 128, cbase);
        if (b < 16) st_word(ws, FL_C + b * 128, cbase);
      }
      wait_vm<0>();
      role_bar(rcnt, 8, l);
      if (tid == 0) st_word(ws, FL_U + b * 128, tag);
    }
    {
      const int c = w >> 1, half = w & 1;
      const int d = dpair * 2 + c;
      const char* tbase[4];
      u32 toff[4];
      {
#pragma unroll
        for (int rr = 0; rr < 4; ++rr) {
          if (w == 0) {
            const int row = 16 * jj + 8 * (rr & 1) + (l >> 3), ln = l & 7;
            int col = 384 * (xcd >> 1) + 64 * (ln > 6 ? 6 : ln);
            col = col > F - 2 ? F - 2 : col;
            tbase[rr] = (const char*)((rr >> 1) ? wu : wg); toff[rr] = (u32)(row * F + col) * 2;
          } else {
            const int ln = CODE_FIRST + (3 * (w - 1) + rr) * 64 + l;
            tbase[rr] = (const char*)pc0; toff[rr] = (u32)(ln < CODE_LINES ? ln : CODE_LINES - 1) * 128;
          }
          asm volatile("" : "+s"(tbase[rr]), "+v"(toff[rr]));
        }
      }
      if (w == 0) poll_words(ws, FL_U + xcd * 128, 8 * 128, 32, tag, l);
      bar();
#pragma unroll
      for (int i = 0; i < 2; ++i)
        dma16<true>(ws + WS_UR + (size_t)d * ST * 2 + (2 * half + i) * 1024, l * 16, sm + L_UR + c * 4608 + (2 * half + i) * 1024);
      wait_vm<0>();
      role_bar(rcnt, 12, l);
      const u32 alo = sm + L_CPY + c * 4 * CSTR + ((li + 1) & 3) * CSTR + 8 * (((li + 1) >> 2) + 2 * g) + 256 * half;
      const u32 bbase = sm + L_UR + c * 4608 + 480 - 32 * li + 16 * g + 256 * half;
      f32x4 acc[8];
#pragma unroll
      for (int n = 0; n < 8; ++n) acc[n] = f32x4{0, 0, 0, 0};
      u32x4 A[2][8], B[2][8];
      auto ldab = [&](int rr, int buf) {
#pragma unroll
        for (int s = 0; s < 8; ++s) {
          const u32x2 lo = lds_ld64v(alo + 512 * s + 64 * rr), hi = lds_ld64v(alo + 512 * s + 64 * rr + 8);
          A[buf][s] = u32x4{lo[0], lo[1], hi[0], hi[1]};
          B[buf][s] = lds_ld128(bbase + 64 * (56 - 8 * s + rr));
        }
      };
      ldab(0, 0);
      static_for<0, 4>([&](auto I) {
        constexpr int rr = I.value;
        __builtin_amdgcn_sched_barrier(0);
        if (w > 0 && w < 3 && rr < 3) dma4(tbase[rr], toff[rr], sm + L_TOUCH);
        if (w == 0 && rr == 3) { for (int q_ = 0; q_ < 4; ++q_) dma4(tbase[q_], toff[q_], sm + L_TOUCH); }
        __builtin_amdgcn_sched_barrier(0);
        if constexpr (rr < 3) ldab(rr + 1, (rr + 1) & 1);
#pragma unroll
        for (int n = 0; n < 8; ++n)
#pragma unroll
          for (int s = 0; s <= n; ++s) acc[n] = mfma16(A[rr & 1][s], B[rr & 1][n - s], acc[n]);
        if constexpr (rr < 3) {
#pragma unroll
          for (int r = 0; r < 12; ++r) {
            __builtin_amdgcn_sched_group_barrier(0x8, 1, 0);
            __builtin_amdgcn_sched_group_barrier(0x100, 1, 0);
            __builtin_amdgcn_sched_group_barrier(0x8, 2, 0);
            __builtin_amdgcn_sched_group_barrier(0x100, 1, 0);
          }
        }
        __builtin_amdgcn_sched_barrier(0);
      });
      f32x4 fin[4];
      const u32 mine = sm + L_RED + w * 4096 + l * 16, theirs = sm + L_RED + (w ^ 1) * 4096 + l * 16;
      if (half == 0) {
        asm volatile("");
#pragma unroll
        for (int nn = 0; nn < 4; ++nn) { lds_st128(mine + nn * 1024, __builtin_bit_cast(u32x4, acc[4 + nn])); fin[nn] = acc[nn]; }
      } else {
        asm volatile("");
#pragma unroll
        for (int nn = 0; nn < 4; ++nn) { lds_st128(mine + nn * 1024, __builtin_bit_cast(u32x4, acc[nn])); fin[nn] = acc[4 + nn]; }
      }
      const int tq = 1024 * half + 16 * li + 4 * g;
      u32x2 ur[4];
#pragma unroll
      for (int nn = 0; nn < 4; ++nn) ur[nn] = lds_ld64(sm + L_UR + c * 4608 + (2044 - tq - 256 * nn) * 2);
      bar();
      f32x4 part[4], rs[4], bs[4];
      u32x2 xe[4];
#pragma unroll
      for (int nn = 0; nn < 4; ++nn) {
        const int t0 = tq + 256 * nn;
        part[nn] = __builtin_bit_cast(f32x4, lds_ld128(theirs + nn * 1024));
        xe[nn] = lds_ld64(sm + L_XR + c * 4096 + t0 * 2);
        rs[nn] = __builtin_bit_cast(f32x4, lds_ld128(sm + L_ST1 + (t0 >> 9) * 8192 + (t0 & 511) * 4));
        bs[nn] = __builtin_bit_cast(f32x4, lds_ld128(sm + L_ST1 + (t0 >> 9) * 8192 + 2048 + (t0 & 511) * 4));
      }
      __builtin_amdgcn_sched_barrier(0);
      u32x2 hp[4];
#pragma unroll
      for (int nn = 0; nn < 4; ++nn) {
        fin[nn] += part[nn];
        const float uv[4] = {bfhi(ur[nn][1]), bflo(ur[nn][1]), bfhi(ur[nn][0]), bflo(ur[nn][0])};
        const float xv[4] = {bflo(xe[nn][0]), bfhi(xe[nn][0]), bflo(xe[nn][1]), bfhi(xe[nn][1])};
        float h[4];
#pragma unroll
        for (int e = 0; e < 4; ++e) {
          const float y = fmaf(uv[e], dD, fin[nn][e]);
          h[e] = fmaf(fmaf(xv[e], rs[nn][e], bs[nn][e]), y, xv[e]);
        }
        hp[nn] = u32x2{pk2(h[0], h[1]), pk2(h[2], h[3])};
        if (nn & 1) {
          const auto s0 = __builtin_amdgcn_permlane16_swap(hp[nn - 1][0], hp[nn][0], false, false);
          const auto s1 = __builtin_amdgcn_permlane16_swap(hp[nn - 1][1], hp[nn][1], false, false);
          const int ts = 256 * (4 * half + nn - 1 + (g & 1)) + 16 * li + 4 * (g & 2);
          bst128<SCP>(rws, WS_HT + (u32)(d * ST + ts) * 2, u32x4{s0[0], s1[0], s0[1], s1[1]});
        }
      }
      wait_vm<0>();
      bar();
      if (w == 0) {
        u64 old = 0;
        if (l == 0) old = add_word(ws, FL_B + xcd * 128);
        old = __shfl(old, 0);
        if (old == cbase + 31 && l < 8) st_word(ws, FL_BR + l * 128 + xcd * 8, tag);
        poll_words(ws, FL_BR + xcd * 128, 8, 8, tag, l);
        if (b == 0 && l == 0) st_word(ws, FL_EPOCH, tag);
      }
      bar();
    }
    {
      auto tra = [&](int cb, int t) -> u32 {
        const int qq = li >> 2, p = l & 3;
        return sm + 512 * g + 64 * (4 * t + qq) + 16 * ((2 * cb + (p >> 1)) ^ ((2 * g + t) & 3)) + 8 * (p & 1);
      };
      u32 wa[3][2], ha[4][2];
#pragma unroll
      for (int j = 0; j < 3; ++j) {
        const int c = wf == 0 ? (j == 0 ? 2 : j - 1) : 3 + j;
#pragma unroll
        for (int t = 0; t < 2; ++t) wa[j][t] = tra(c & 1, t) + ((c >> 1) < 2 ? 512 * g + 512 * (c >> 1) : 4096);
      }
#pragma unroll
      for (int j = 0; j < 4; ++j) {
        const int tb = 4 * wt + ((j + 2 * wf) & 3);
#pragma unroll
        for (int t = 0; t < 2; ++t) ha[j][t] = tra(j & 1, t) + 1536 * g + 12288 + 512 * (tb >> 1);
      }
      auto slot_step = [&](int it) -> u32 { return it % G1_NSLOT == 0 ? (u32)(-(G1_NSLOT - 1) * G1_STAGE) : (u32)G1_STAGE; };
      if (g1_live) {
        auto advance = [&](int it) {
          const u32 dlt = slot_step(it);
#pragma unroll
          for (int t = 0; t < 2; ++t) {
#pragma unroll
            for (int j = 0; j < 3; ++j) { asm volatile("" : "+v"(wa[j][t])); wa[j][t] += dlt; }
#pragma unroll
            for (int j = 0; j < 4; ++j) { asm volatile("" : "+v"(ha[j][t])); ha[j][t] += dlt; }
          }
        };
        f32x4 accg[3][4], accu[3][4];
#pragma unroll
        for (int j = 0; j < 3; ++j)
#pragma unroll
          for (int n = 0; n < 4; ++n) { accg[j][n] = f32x4{0, 0, 0, 0}; accu[j][n] = f32x4{0, 0, 0, 0}; }
        u32x4 fg[2][3], fu[2][3], fh[2][4];
        auto ldfrag = [&](int buf) {
#pragma unroll
          for (int j = 0; j < 4; ++j) {
            const u32x2 a = lds_tr(ha[j][0]), c = lds_tr(ha[j][1]);
            fh[buf][j] = u32x4{a[0], a[1], c[0], c[1]};
          }
#pragma unroll
          for (int j = 0; j < 3; ++j) {
            u32x2 a = lds_tr(wa[j][0]), c = lds_tr(wa[j][1]);
            fg[buf][j] = u32x4{a[0], a[1], c[0], c[1]};
            a = lds_tr(wa[j][0] + 6144); c = lds_tr(wa[j][1] + 6144);
            fu[buf][j] = u32x4{a[0], a[1], c[0], c[1]};
          }
        };
        u32 wdoff;
        { const int ln0 = w * 64 + l, ln = jj * 172 + (ln0 < 172 ? ln0 : 171); wdoff = (u32)((ln >> 2) * 1024 + (4 * (xcd & 1) + (ln & 3)) * 128); asm volatile("" : "+v"(wdoff)); }
        bar();
        ldfrag(0);
        advance(1);
        bar();
        static_for<0, G1_NIT>([&](auto I) {
          constexpr int it = I.value;
          __builtin_amdgcn_sched_barrier(0);
          if constexpr (it + 1 < G1_NIT) {
            if constexpr (it > 0 && (it & 1) == 0) bar();
            if constexpr (it == 7) dma4(wd, wdoff, sm + L_TOUCH);
            ldfrag((it + 1) & 1);
            if constexpr (it + 2 < G1_NIT) advance(it + 2);
          }
#pragma unroll
          for (int j = 0; j < 3; ++j)
#pragma unroll
            for (int n = 0; n < 4; ++n) {
              accg[j][n] = mfma16(fg[it & 1][j], fh[it & 1][n], accg[j][n]);
              accu[j][n] = mfma16(fu[it & 1][j], fh[it & 1][n], accu[j][n]);
            }
          if constexpr (it + 1 < G1_NIT) {
#pragma unroll
            for (int r = 0; r < 10; ++r) {
              __builtin_amdgcn_sched_group_barrier(0x8, 1, 0);
              __builtin_amdgcn_sched_group_barrier(0x100, 2, 0);
            }
#pragma unroll
            for (int r = 0; r < 14; ++r) {
              __builtin_amdgcn_sched_group_barrier(0x8, 1, 0);
              __builtin_amdgcn_sched_group_barrier(0x2, 1, 0);
            }
          }
        });
        __builtin_amdgcn_sched_barrier(0);
        bar();
        f32x4 csg[3], csu[3];
#pragma unroll
        for (int j = 0; j < 3; ++j) {
          const int c = wf == 0 ? (j == 0 ? 2 : j - 1) : 3 + j;
          csg[j] = *(LAS f32x4*)(sm + L_CS + (16 * c + 4 * g) * 4);
          csu[j] = *(LAS f32x4*)(sm + L_CS + 384 + (16 * c + 4 * g) * 4);
        }
        u32x2 mid[2];
        f32x4 st4[4];
#pragma unroll
        for (int n = 0; n < 4; ++n) st4[n] = __builtin_bit_cast(f32x4, lds_ld128(sm + L_ST2 + ((4 * wt + ((n + 2 * wf) & 3)) * 16 + li) * 16));
#pragma unroll
        for (int nn = 0; nn < 4; ++nn) {
          const int n = nn ^ 2;
          const f32x4 ag[3] = {accg[0][n], accg[1][n], accg[2][n]}, au[3] = {accu[0][n], accu[1][n], accu[2][n]};
          u32 pk[6];
          glu(n, st4[n], ag, au, csg, csu, pk);
          if (n >= 2) lds_st64(sm + L_MID + (w * 2 + n - 2) * 512 + l * 8, u32x2{pk[0], pk[1]});
          else mid[n] = u32x2{pk[0], pk[1]};
          if (n == 3) { __builtin_amdgcn_s_waitcnt(0xC07F); lds_signal(sm + L_CNT + 12, l); }
        }
        lds_wait(sm + L_CNT + 12, 4);
        const int ks_mid = 3 * nt1 + 1;
        const u32 o_mid = ks_mid < NKS ? (u32)ks_mid * 1024 : 0x80000000u;
#pragma unroll
        for (int n = 0; n < 2; ++n) {
          const u32x2 o = lds_ld64(sm + L_MID + ((w ^ 1) * 2 + n) * 512 + l * 8);
          const int tb = 4 * wt + 2 * wf + n;
          bst128<SCP>(rws, WS_HH + (u32)((8 * mt1 + tb) * NKS) * 1024 + l * 16 + o_mid,
                      wf == 0 ? u32x4{mid[n][0], mid[n][1], o[0], o[1]} : u32x4{o[0], o[1], mid[n][0], mid[n][1]});
        }
        lds_signal(sm + L_CNT + 8, l);
        wait_vm<0>();
        role_bar(rcnt, 16, l);
        if (tid == 0) (void)add_word(ws, FL_C + mt1 * 128);
      } else {
        { const int ln0 = w * 64 + l, ln = jj * 172 + (ln0 < 172 ? ln0 : 171); dma4(wd, (u32)((ln >> 2) * 1024 + (4 * (xcd & 1) + (ln & 3)) * 128), sm + L_TOUCH); }
        f32x4 msum[2] = {f32x4{0, 0, 0, 0}, f32x4{0, 0, 0, 0}}, msq[2] = {f32x4{0, 0, 0, 0}, f32x4{0, 0, 0, 0}};
        static_for<0, G1_NIT>([&](auto I) {
          constexpr int it = I.value;
          constexpr u32 so = (it % G1_NSLOT) * G1_STAGE;
          bar();
#pragma unroll
          for (int n = 0; n < 2; ++n) {
            const u32x2 a = lds_tr(ha[n][0] + so), c = lds_tr(ha[n][1] + so);
            const u32x4 f = {a[0], a[1], c[0], c[1]};
            msum[n] = mfma16(u32x4{0x3f803f80u, 0x3f803f80u, 0x3f803f80u, 0x3f803f80u}, f, msum[n]);
            msq[n] = mfma16(f, f, msq[n]);
          }
        });
#pragma unroll
        for (int n = 0; n < 2; ++n) {
          const int e = li & 3;
          const float dg = e == 0 ? msq[n][0] : e == 1 ? msq[n][1] : e == 2 ? msq[n][2] : msq[n][3];
          const float ssq = __shfl(dg, 16 * (li >> 2) + li);
          const float m = msum[n][0] * (1.f / D);
          const float var = fmaxf(ssq * (1.f / D) - m * m, 0.f);
          const int t = mt1 * 128 + (4 * wt + 2 * wf + n) * 16 + li;
          if (l < 32)
            st_word(ws, WS_ST2 + t * 16 + (g & 1) * 8, ((u64)(u32)tag << 32) | __float_as_uint((g & 1) ? m : rsqrtf(var + EPS)));
        }
        bar();
        lds_signal(sm + L_CNT + 8, l);
      }
    }
    bar();
    static_for<0, 22>([&](auto I) { g2_read_w(I.value, I.value); });
    if (w == 0) poll_words(ws, FL_C + (mt2 >> 1) * 128, 128, 1, cbase + 15, l);
    bar();
  }

  {
    const int kh = w8 >> 2, wt = w >> 1;
    const char* hp = ws + WS_HH + (size_t)(4 * mt2 + w) * NKS * 1024;
    auto stage_h = [&](int s) {
#pragma unroll
      for (int kk = 0; kk < 2; ++kk) {
        int ks = 4 * s + 2 * kk + kh;
        const int slot = ks % G2_RING;
        const int ksc = ks < NKS ? ks : NKS - 2;
        dma16<true>(hp + (size_t)ksc * 1024, l * 16, sm + (ks < NKS ? slot : (NKS - 2) % G2_RING) * 4096 + w * 1024);
      }
    };
    static_for<0, G2_PF>([&](auto Q) { stage_h(Q.value); });
    auto issue = [&](auto I) {
      constexpr int s = I.value;
      static_for<g2_issued(s), g2_issued(s + 1)>([&](auto Q) { stage_h(Q.value); });
    };
    f32x4 acc[2][2];
#pragma unroll
    for (int i = 0; i < 2; ++i)
#pragma unroll
      for (int n = 0; n < 2; ++n) acc[i][n] = f32x4{0, 0, 0, 0};
    u32x4 fh[2][2][2];
    const u32 ha = sm + (2 * wt) * 1024 + kh * 4096 + l * 16;
    static_for<0, G2_NST + 1>([&](auto I) {
      constexpr int s = I.value;
      __builtin_amdgcn_sched_barrier(0);
      if constexpr (s < G2_NST) {
        if constexpr ((s & 1) == 0) {
          constexpr int last = g2_issued(s) - 1;
          constexpr int need = s + 1 < G2_NST ? s + 1 : s;
          wait_vm<2 * (last - need)>();
          bar();
        }
#pragma unroll
        for (int kk = 0; kk < 2; ++kk)
#pragma unroll
          for (int n = 0; n < 2; ++n)
            if (4 * s + 2 * kk < NKS) fh[s & 1][kk][n] = lds_ld128(ha + ((4 * s + 2 * kk) % G2_RING) * 4096 + n * 1024);
        __builtin_amdgcn_sched_barrier(0);
        if (kh == 0) issue(I);
        __builtin_amdgcn_sched_barrier(0);
      }
      if constexpr (s >= 1) {
#pragma unroll
        for (int kk = 0; kk < 2; ++kk)
          if (4 * (s - 1) + 2 * kk < NKS) {
#pragma unroll
            for (int i = 0; i < 2; ++i)
#pragma unroll
              for (int n = 0; n < 2; ++n) acc[i][n] = mfma16(fw[2 * (s - 1) + kk][i], fh[(s - 1) & 1][kk][n], acc[i][n]);
          }
      }
      if constexpr (s < G2_NST) { __builtin_amdgcn_sched_barrier(0); if (kh == 1) issue(I); }
    });
    __builtin_amdgcn_sched_barrier(0);
    const u32 mine = sm + L_OX + w8 * 2048 + l * 16, theirs = sm + L_OX + (w8 ^ 4) * 2048 + l * 16;
    f32x4 fin[2];
    if (kh == 0) {
      asm volatile("");
#pragma unroll
      for (int i = 0; i < 2; ++i) { lds_st128(mine + i * 1024, __builtin_bit_cast(u32x4, acc[i][1])); fin[i] = acc[i][0]; }
    } else {
      asm volatile("");
#pragma unroll
      for (int i = 0; i < 2; ++i) { lds_st128(mine + i * 1024, __builtin_bit_cast(u32x4, acc[i][0])); fin[i] = acc[i][1]; }
    }
    const int tb = 2 * wt + kh;
    u32x2 r[2];
#pragma unroll
    for (int i = 0; i < 2; ++i) {
      const int qq = li >> 2, p = l & 3;
      const int row = 32 * (w & 1) + 8 * g + 4 * i + qq;
      const int c4 = 2 * (tb & 1) + (p >> 1);
      r[i] = lds_tr(sm + L_RES + 1024 * (row >> 3) + 512 * (tb >> 1) + 64 * (row & 7) + 16 * (c4 ^ ((row >> 2) & 3)) + 8 * (p & 1));
    }
    bar();
#pragma unroll
    for (int i = 0; i < 2; ++i) fin[i] += __builtin_bit_cast(f32x4, lds_ld128(theirs + i * 1024));
    const f32x4 lo = fin[0], hi = fin[1];
    const u32x4 o = {pk2(lo[0] + bflo(r[0][0]), lo[1] + bfhi(r[0][0])), pk2(lo[2] + bflo(r[0][1]), lo[3] + bfhi(r[0][1])),
                     pk2(hi[0] + bflo(r[1][0]), hi[1] + bfhi(r[1][0])), pk2(hi[2] + bflo(r[1][1]), hi[3] + bfhi(r[1][1]))};
    *(u32x4*)(out + (size_t)(mt2 * 64 + 16 * tb + li) * D + nt2 * 64 + 32 * (w & 1) + 8 * g) = o;
  }
}

extern "C" void kernel_launch(void* const* d_in, const int* in_sizes, int n_in, void* d_out, int out_size,
                              void* d_ws, size_t ws_size, hipStream_t stream) {
  fused<<<256, 512, 0, stream>>>((const u16*)d_in[0], (const u16*)d_in[1], (const u16*)d_in[2], (const u16*)d_in[3],
                                 (const u16*)d_in[4], (const u16*)d_in[5], (const u16*)d_in[7], (const u16*)d_in[8],
                                 (const u16*)d_in[9], (u16*)d_out, (char*)d_ws);
}
```
